```python
import torch

def _softmax(x, axis=-1):
    return torch.softmax(x, dim=axis)
def _gelu(x):
    return torch.nn.functional.gelu(x, approximate="tanh")
def _silu(x):
    return torch.nn.functional.silu(x)
def _sigmoid(x):
    return torch.sigmoid(x)
def _layernorm(x, axis=-1, eps=1e-5):
    m = x.mean(dim=axis, keepdim=True)
    v = x.var(dim=axis, keepdim=True, unbiased=False)
    return (x - m) / torch.sqrt(v + eps)
def _scatter_add(tgt, idx, src, axis=0):
    return tgt.clone().index_add_(axis, idx, src)
def _segment_sum(src, seg, n):
    out = torch.zeros((n,) + src.shape[1:], dtype=src.dtype)
    return out.index_add_(0, seg, src)
def _pad(x, pw):
    flat = tuple(v for pair in reversed(pw) for v in pair)
    return torch.nn.functional.pad(x, flat)

kernel_name = 'gated_mix__sliding_local_dilated__dense_activation_squared_relu__prompt'

def setup_inputs():

    _w = (lambda a: (lambda t: t.to(torch.bfloat16) if t.is_floating_point() else t)(torch.as_tensor(a)))
    def _A_setup():
        B, S, D = 1, 2048, 448
        return {
            "x": _w((lambda *s: torch.randn(*s, dtype=torch.bfloat16) * 0.05)(B, S, D)),
            "wqkv": _w((lambda *s: torch.randn(*s, dtype=torch.bfloat16) * 0.8)(D, 3 * D)),
            "wo": _w((lambda *s: torch.randn(*s, dtype=torch.bfloat16) * 0.05)(D, D)),
        }
    _A_in = _A_setup()

    def _B_setup():
        B, S, D, F = 1, 2048, 448, 1792
        return {
            "x": _w((lambda *s: torch.randn(*s, dtype=torch.bfloat16) * 0.05)(B, S, D)),
            "w1": _w((lambda *s: torch.randn(*s, dtype=torch.bfloat16) * 0.1)(D, F)),
            "b1": _w((lambda *s: torch.randn(*s, dtype=torch.bfloat16) * 0.1)(F)),
            "w2": _w((lambda *s: torch.randn(*s, dtype=torch.bfloat16) * 0.1)(F, D)),
            "b2": _w((lambda *s: torch.randn(*s, dtype=torch.bfloat16) * 0.1)(D)),
        }
    _B_in = _B_setup()

    return {
        "A_x": _A_in["x"],
        "A_wqkv": _A_in["wqkv"],
        "A_wo": _A_in["wo"],
        "B_x": _B_in["x"],
        "B_w1": _B_in["w1"],
        "B_b1": _B_in["b1"],
        "B_w2": _B_in["w2"],
        "B_b2": _B_in["b2"],
        "gate_w": _w((lambda *s: torch.randn(*s, dtype=torch.bfloat16) * 0.05)(448) * 16.0),
    }

def reference(A_x, A_wqkv, A_wo, B_x, B_w1, B_b1, B_w2, B_b2, gate_w):
    def _A_ref(x, wqkv, wo):
        B, S, D = x.shape
        H = 16; Dh = D // H
        W, dil = 8, 2
        qkv = (lambda x, *d: x.permute(*d))((x @ wqkv).reshape(B, S, 3, H, Dh), 2, 0, 3, 1, 4)
        q, k, v = qkv[0], qkv[1], qkv[2]
        scale = Dh ** -0.5
        pos = torch.arange(S)
        off = torch.arange(W) * dil
        idx = pos[:, None] - off[None, :]
        mask = idx >= 0
        idx_c = torch.where(mask, idx, 0).reshape(-1)
        kg = k[:, :, idx_c, :].reshape(B, H, S, W, Dh)
        vg = v[:, :, idx_c, :].reshape(B, H, S, W, Dh)
        logits = torch.einsum("bhsd,bhswd->bhsw", q, kg) * scale
        logits = torch.where(mask[None, None, :, :], logits, -1e9)
        attn = _softmax(logits, axis=-1)
        o = torch.einsum("bhsw,bhswd->bhsd", attn, vg)
        return (lambda x, *d: x.permute(*d))(o, 0, 2, 1, 3).reshape(B, S, D) @ wo

    def _B_ref(x, w1, b1, w2, b2):
        h = torch.einsum("bsd,df->bsf", x, w1) + b1
        zero = torch.zeros_like(h)
        r = torch.where(h > zero, h, zero)
        h = r * r
        out = torch.einsum("bsf,fd->bsd", h, w2) + b2
        return out

    x = A_x
    g = _sigmoid((x * gate_w).sum(-1, keepdims=True))
    a = _A_ref(x, A_wqkv, A_wo)
    b = _B_ref(x, B_w1, B_b1, B_w2, B_b2)
    return g * a + (1 - g) * b
```

```cpp
#include <hip/hip_runtime.h>
#include <hip/hip_bf16.h>
#include <stdint.h>

typedef __bf16 bf16_t;
typedef __attribute__((ext_vector_type(8))) __bf16 bf16x8;
typedef __attribute__((ext_vector_type(4))) __bf16 bf16x4;
typedef __attribute__((ext_vector_type(2))) __bf16 bf16x2;
typedef __attribute__((ext_vector_type(4))) float f32x4;
typedef __attribute__((ext_vector_type(2))) float f32x2;
typedef __attribute__((ext_vector_type(4))) unsigned u32x4;
typedef __attribute__((ext_vector_type(2))) unsigned u32x2;
typedef __attribute__((ext_vector_type(4))) int i32x4;
typedef unsigned long long u64;
#define LDSP(T) __attribute__((address_space(3))) T*
#define GLBP(T) __attribute__((address_space(1))) T*
#define DI __device__ __forceinline__

constexpr int S = 2048, D = 448, F = 1792;
constexpr int KS2 = 72;
constexpr unsigned CODE_BYTES = 24064;
constexpr size_t ACT_BYTES = (size_t)32 * KS2 * 4096;
constexpr size_t WS_GATE = ACT_BYTES, WS_MAIL = WS_GATE + 128 * 128, WS_EPOCH = WS_MAIL + 224 * 256;

DI i32x4 make_rsrc(const void* p, unsigned bytes) {
  uint64_t a = (uint64_t)p;
  i32x4 r; r.x = (int)(unsigned)a; r.y = (int)((unsigned)(a >> 32) & 0xffffu); r.z = (int)bytes; r.w = 0x00020000; return r;
}
DI void dma16(unsigned lds, unsigned voff, i32x4 rs, unsigned soff) {
  asm volatile("s_mov_b32 m0, %0\n\ts_nop 0\n\tbuffer_load_dwordx4 %1, %2, %3 offen lds" :: "s"(lds), "v"(voff), "s"(rs), "s"(soff) : "memory");
}
DI void dma16m(unsigned lds, unsigned voff, i32x4 rs, unsigned soff, u64 mask) {
  asm volatile("s_mov_b32 m0, %0\n\ts_mov_b64 exec, %4\n\tbuffer_load_dwordx4 %1, %2, %3 offen lds\n\ts_mov_b64 exec, -1"
               :: "s"(lds), "v"(voff), "s"(rs), "s"(soff), "s"(mask) : "memory");
}
template <int N> DI void wait_vm() { asm volatile("s_waitcnt vmcnt(%0)" :: "i"(N) : "memory"); }
template <int N> DI void wait_vm_bar() { asm volatile("s_waitcnt vmcnt(%0)\n\ts_barrier" :: "i"(N) : "memory"); }
DI void lgkm_bar() { asm volatile("s_waitcnt lgkmcnt(0)\n\ts_barrier" ::: "memory"); }
DI void bar() { asm volatile("s_barrier" ::: "memory"); }
DI void bstore16_dev(u32x4 v, unsigned voff, i32x4 rs, unsigned soff) {
  asm volatile("buffer_store_dwordx4 %0, %1, %2, %3 offen sc1\n\ts_nop 1" :: "v"(v), "v"(voff), "s"(rs), "s"(soff) : "memory");
}
DI void aload2(u32x4& d0, u32x4& d1, unsigned voff, i32x4 rs, unsigned soff) {
  asm volatile("buffer_load_dwordx4 %0, %2, %3, %4 offen sc1\n\tbuffer_load_dwordx4 %1, %2, %3, %4 offen offset:1024 sc1"
               : "=&v"(d0), "=&v"(d1) : "v"(voff), "s"(rs), "s"(soff) : "memory");
}
template <int N> DI void wait_a2(u32x4& d0, u32x4& d1) { asm volatile("s_waitcnt vmcnt(%2)" : "+v"(d0), "+v"(d1) : "i"(N) : "memory"); }
DI u64 ld_dev(const u64* p) { return __hip_atomic_load((GLBP(u64))p, __ATOMIC_RELAXED, __HIP_MEMORY_SCOPE_AGENT); }
DI void st_dev(u64* p, u64 v) { __hip_atomic_store((GLBP(u64))p, v, __ATOMIC_RELAXED, __HIP_MEMORY_SCOPE_AGENT); }
DI void wait_flags(const u64* p, u64 tag) {
#pragma unroll 1
  for (int spin = 0; spin < (1 << 16); ++spin) {
    u64 v = ld_dev(p);
    if (__all(v == tag)) break;
    __builtin_amdgcn_s_sleep(1);
  }
  asm volatile("" ::: "memory");
}
DI float relu(float a) { int i = __builtin_bit_cast(int, a); return __builtin_bit_cast(float, i > 0 ? i : 0); }
DI float max3(float a, float b, float c) { return __builtin_fmaxf(__builtin_fmaxf(a, b), c); }
DI float xmax16(float v) { unsigned u = __builtin_bit_cast(unsigned, v); auto r = __builtin_amdgcn_permlane16_swap(u, u, false, false);
  unsigned a = r[0], b = r[1]; return max3(__builtin_bit_cast(float, a), __builtin_bit_cast(float, b), -3.0e38f); }
DI float xmax32(float v) { unsigned u = __builtin_bit_cast(unsigned, v); auto r = __builtin_amdgcn_permlane32_swap(u, u, false, false);
  unsigned a = r[0], b = r[1]; return max3(__builtin_bit_cast(float, a), __builtin_bit_cast(float, b), -3.0e38f); }
DI bf16x4 tr_read(unsigned addr) { return __builtin_amdgcn_ds_read_tr16_b64_v4bf16((LDSP(bf16x4))(uintptr_t)addr); }
DI bf16x8 tr_frag(unsigned a0, unsigned a1) { bf16x4 lo = tr_read(a0), hi = tr_read(a1); return __builtin_shufflevector(lo, hi, 0, 1, 2, 3, 4, 5, 6, 7); }
DI bf16x8 lds_frag(unsigned addr) { return *(LDSP(bf16x8))(uintptr_t)addr; }
DI f32x4 lds_f4(unsigned addr) { return *(LDSP(f32x4))(uintptr_t)addr; }
DI f32x4 mfma(bf16x8 a, bf16x8 b, f32x4 c) { return __builtin_amdgcn_mfma_f32_16x16x32_bf16(a, b, c, 0, 0, 0); }
DI bf16x8 asb(u32x4 v) { return __builtin_bit_cast(bf16x8, v); }
DI unsigned pk2(float a, float b) { f32x2 v = {a, b}; bf16x2 r = __builtin_convertvector(v, bf16x2); return __builtin_bit_cast(unsigned, r); }
DI u32x4 pack8(f32x4 a, f32x4 b) { return u32x4{pk2(a[0], a[1]), pk2(a[2], a[3]), pk2(b[0], b[1]), pk2(b[2], b[3])}; }
DI float bf_lo(unsigned v) { return __builtin_bit_cast(float, v << 16); }
DI float bf_hi(unsigned v) { return __builtin_bit_cast(float, v & 0xffff0000u); }
DI f32x4 bf4(u32x2 bb) { return f32x4{bf_lo(bb.x), bf_hi(bb.x), bf_lo(bb.y), bf_hi(bb.y)}; }
template <int SI> struct IC { static constexpr int v = SI; };
template <int I, int N, class Fn> DI void static_for(Fn&& f) { if constexpr (I < N) { f(IC<I>{}); static_for<I + 1, N>(f); } }
template <int NM, int ND> DI void interleave() {
#pragma unroll
  for (int i = 0; i < NM; ++i) { __builtin_amdgcn_sched_group_barrier(0x8, 1, 0); if (i < ND) __builtin_amdgcn_sched_group_barrier(0x100, 1, 0); }
}

constexpr unsigned XS0 = 131072, XS1 = 32768, XS2 = 98304;
constexpr unsigned WS0 = 98304 + 17408, WS1 = 131072 + 17408, WS2 = 65536, WS3 = 65536 + 14336;
constexpr unsigned LDS_G = 131072 + 17408 + 14336;
DI unsigned next_x(unsigned xs) { return xs == XS0 ? XS1 : (xs == XS1 ? XS2 : XS0); }

__global__ __launch_bounds__(512, 2) void fused_kernel(const bf16_t* __restrict__ x, const bf16_t* __restrict__ wqkv, const bf16_t* __restrict__ wo,
                                                       const bf16_t* __restrict__ w1, const bf16_t* __restrict__ b1, const bf16_t* __restrict__ w2,
                                                       const bf16_t* __restrict__ b2, const bf16_t* __restrict__ gw, char* ws, bf16_t* __restrict__ out) {
  __shared__ __attribute__((aligned(1024))) char smem[163840];
  const unsigned lds0 = (unsigned)(uintptr_t)(LDSP(char))smem;
  {
    u64 pc; asm volatile("s_getpc_b64 %0" : "=s"(pc));
    const unsigned co = min(((blockIdx.x >> 3) & 3) * 8192 + (threadIdx.x >> 3) * 128, CODE_BYTES);
    asm volatile("s_mov_b32 m0, %0\n\ts_nop 0\n\tglobal_load_lds_dword %1, %2" :: "s"(lds0), "v"(co), "s"(pc));
  }
  asm volatile("" :: "s"(x), "s"(wqkv), "s"(wo), "s"(w1), "s"(b1), "s"(w2), "s"(b2), "s"(gw), "s"(ws), "s"(out));
  const int b = blockIdx.x, xcd = b & 7, idx = b >> 3;
  const int grp = (xcd & 1) * 8 + (idx >> 2), sub = (xcd >> 1) * 2 + (idx & 1);
  const bool mlp = (idx >> 1) & 1;
  const int wv = __builtin_amdgcn_readfirstlane(threadIdx.x >> 6), l = threadIdx.x & 63;
  const int w = wv & 3;
  const bool older = wv < 4;
  const int tile = (mlp ? 0 : 16) + (idx >> 2) * 2 + (idx & 1);
  const bool has2 = tile < 28;
  const int rbk = xcd * 4 + tile / 7, ctile = tile % 7, cons = rbk * 7 + ctile;
  const int g = l >> 4, t = l & 15, q = (l >> 2) & 3, p = l & 3;
  const f32x4 zero4 = {0.f, 0.f, 0.f, 0.f};
  const i32x4 rs2 = make_rsrc(w2, has2 ? F * D * 2 : 0), rso = make_rsrc(wo, D * D * 2);
  unsigned bo = 0;
  auto issue_b = [&](int u0, int n) {
#pragma unroll
    for (int u = u0; u < u0 + n; ++u) {
      if (u < 40) dma16(lds0 + w * 1024 + u * 4096, bo, rs2, u * 32 * 896);
      else if ((u & 3) < 2) dma16(lds0 + w * 1024 + (u - 40) * 4096, bo, rs2, (40 + 2 * ((u - 40) >> 2) + (u & 3)) * 32 * 896);
      else dma16(lds0 + w * 1024 + (u - 40) * 4096, bo, rso, (2 * ((u - 40) >> 2) + (u & 3) - 2) * 28 * 896);
    }
  };
  u64 tagv = 0;
  u64* flagp = nullptr;

  if (__builtin_expect(!older, 1)) {
    const int tok0 = (grp >> 1) * 256 + (grp & 1);
    const i32x4 rsx = make_rsrc(x, S * D * 2);
    unsigned xo[4];
#pragma unroll
    for (int ii = 0; ii < 4; ++ii) { int r = 32 * w + 8 * ii + (l >> 3); xo[ii] = (unsigned)((tok0 + 2 * r) * 896 + (((l & 7) ^ ((r >> 1) & 7)) << 4)); }
    const unsigned xdst = lds0 + w * 4096;
    auto issue_xa = [&](int j, unsigned slot) { dma16(xdst + slot, xo[0], rsx, j * 128); dma16(xdst + slot + 1024, xo[1], rsx, j * 128); };
    auto issue_xb = [&](int j, unsigned slot) { dma16(xdst + slot + 2048, xo[2], rsx, j * 128); dma16(xdst + slot + 3072, xo[3], rsx, j * 128); };
    issue_xa(0, XS0); issue_xb(0, XS0);
    const i32x4 rsw = mlp ? make_rsrc(w1, D * F * 2) : make_rsrc(wqkv, D * 3 * D * 2);
    const i32x4 rs4 = mlp ? rsw : rsx;
    unsigned wo_, v4a, v4b;
    {
      const int l28 = l >= 28 ? 1 : 0, pos = l - 28 * l28;
      int wch = (l & 7) ^ (((l >> 4) & 1) * 2 + (w & 1) * 4); wch = wch > 6 ? 6 : wch;
      wo_ = mlp ? (unsigned)((8 * w + l28) * 3584 + sub * 448 + ((pos ^ ((w & 1) << 1)) << 4)) : (unsigned)((8 * w + (l >> 3)) * 2688 + sub * 112 + wch * 16);
      const int tk = tok0 + 2 * (2 * w - 8), ho = ((l & 7) ^ (2 * w)) << 4;
      v4a = mlp ? wo_ : (unsigned)((tk < 0 ? (grp & 1) : tk) * 896 + ho);
      v4b = mlp ? wo_ : (unsigned)((tk < 0 ? (grp & 1) : tk + 2) * 896 + ho);
    }
    const u64 ex3 = mlp ? 0x00ffffffffffffffull : ~0ull, ex4 = mlp ? 0x00ffffffffffffffull : 0xffull;
    const unsigned wdst = lds0 + w * (mlp ? 3584 : 3072), dstep = mlp ? 896 : 1024, sstep = mlp ? 2 * 3584 : 896, kstep = mlp ? 32 * 3584 : 32 * 2688;
    const unsigned hdst = lds0 + 16384 + w * 256;
    auto issue_w = [&](int s, unsigned slot, unsigned v4, int j, unsigned xslot) {
      const unsigned so = (unsigned)s * kstep;
#pragma unroll
      for (int ii = 0; ii < 3; ++ii) dma16m(wdst + slot + ii * dstep, wo_, rsw, so + ii * sstep, ex3);
      dma16m(mlp ? wdst + slot + 3 * 896 : hdst + xslot, v4, rs4, mlp ? so + 3 * 2 * 3584 : (unsigned)j * 128u, ex4);
    };
    if (!mlp) {
      dma16m(hdst + XS0, v4a, rsx, 0, 0xffull);
      dma16m(lds0 + LDS_G, l * 16, make_rsrc(gw, 896), 0, 0x00ffffffffffffffull);
    }
    issue_w(0, WS0, v4b, 0, XS0 + 128); issue_xa(1, XS1); issue_w(1, WS1, v4a, 1, XS1); issue_xb(1, XS1); issue_w(2, WS2, v4b, 1, XS1 + 128);
    bo = (unsigned)((8 * w + (l >> 3)) * 896 + ctile * 128 + (((l & 7) ^ (((l >> 4) & 3) << 1)) << 4));
    wait_vm_bar<12>();
    unsigned xs = XS2;
#pragma unroll 1
    for (int j = 0; j < 5; ++j) {
      issue_xa(j + 2, xs); issue_w(2 * j + 3, (j & 1) ? WS1 : WS3, v4a, j + 2, xs);
      wait_vm_bar<12>();
      issue_xb(j + 2, xs); issue_w(2 * j + 4, (j & 1) ? WS2 : WS0, v4b, j + 2, xs + 128);
      wait_vm_bar<12>();
      xs = next_x(xs);
    }
    issue_w(13, WS1, v4b, 6, XS0 + 128); issue_b(0, 2);
    wait_vm_bar<12>();
    issue_b(2, 6);
    wait_vm_bar<12>();
    issue_b(8, 6);
    wait_vm_bar<14>();
    if (!has2) { bar(); wait_vm<0>(); return; }
    issue_b(14, 6);
    bar();
    issue_b(20, 20);
    wait_vm<0>();
  } else {
    __builtin_amdgcn_s_setprio(2);
    u32x2 bias[8];
    if (mlp) {
      const bf16_t* bp = b1 + sub * 224 + 4 * g; const bf16_t* bq = bp + 128 * (w & 1);
      asm volatile("global_load_dwordx2 %0, %8, off\n\tglobal_load_dwordx2 %1, %8, off offset:32\n\tglobal_load_dwordx2 %2, %8, off offset:64\n\t"
                   "global_load_dwordx2 %3, %8, off offset:96\n\tglobal_load_dwordx2 %4, %8, off offset:128\n\tglobal_load_dwordx2 %5, %8, off offset:160\n\t"
                   "global_load_dwordx2 %6, %9, off offset:192\n\tglobal_load_dwordx2 %7, %9, off offset:224"
                   : "=&v"(bias[0]), "=&v"(bias[1]), "=&v"(bias[2]), "=&v"(bias[3]), "=&v"(bias[4]), "=&v"(bias[5]), "=&v"(bias[6]), "=&v"(bias[7])
                   : "v"(bq), "v"(bp) : "memory");
    }
    const int half = w >> 1;
    const int cidx = l < 7 ? (2 * grp + half) * 7 + l : (has2 ? cons : 0);
    { const u64* ep = (const u64*)(ws + WS_EPOCH) + cidx * 16;
      asm volatile("global_load_dwordx2 %0, %1, off sc1" : "+v"(tagv) : "v"(ep) : "memory"); }
    const char* tp;
    {
      const int kb0 = w == 0 ? 1 : 4;
      int id = b * (w == 2 ? 62 : 61) + (l < 61 ? l : (w == 2 ? 61 : 60));
      if (w == 2) { id = id < 15680 ? id : 15679; tp = id < 12544 ? (const char*)w2 + (size_t)id * 128 : (const char*)wo + (size_t)(id - 12544) * 128; }
      else if (id < 5376) tp = (const char*)w1 + (size_t)(kb0 * 1792 + id) * 128;
      else if (id < 9408) tp = (const char*)wqkv + (size_t)(kb0 * 1344 + id - 5376) * 128;
      else { int i2 = (id < 15552 ? id : 15551) - 9408; int r = i2 / 3; tp = (const char*)x + (size_t)r * 896 + (kb0 + i2 - 3 * r) * 128; }
    }
    const int tpos = w == 0 ? -1 : (w == 1 ? 0 : (w == 2 ? 2 : 99));
    auto touch = [&](int j) {
      if (j == tpos) asm volatile("s_mov_b32 m0, %0\n\ts_nop 0\n\tglobal_load_lds_dword %1, off" :: "s"(lds0 + 256u), "v"(tp));
      if (j == 4) wait_vm<0>();
    };
    touch(-1);
    const unsigned xsw = (t >> 1) & 7;
    flagp = (u64*)(ws + WS_MAIL) + cidx * 32 + (mlp ? 16 : 0) + sub * 2 + (w & 1);
    if (mlp) {
      const int wf = w & 1;
      const unsigned wb0 = lds0 + (8 * g + q) * 448 + 8 * p;
      const unsigned gsw = (g & 1) ? 32 : 0;
      const unsigned wbE = wb0 + 256 * wf + gsw, wbO = wb0 + 256 * wf - gsw, wsE = wb0 + gsw, wsO = wb0 - gsw;
      const unsigned xr = lds0 + (64 * half + t) * 128 + ((g ^ xsw) << 4);
      const unsigned xA = xr + 4096 * wf, xB = xr + 4096 * (1 - wf);
      f32x4 acc[6][4], acs[2][2];
      asm volatile("s_waitcnt vmcnt(1)" : "+v"(bias[0]), "+v"(bias[1]), "+v"(bias[2]), "+v"(bias[3]), "+v"(bias[4]), "+v"(bias[5]), "+v"(bias[6]), "+v"(bias[7]) :: "memory");
#pragma unroll
      for (int u = 0; u < 6; ++u) { f32x4 bv = bf4(bias[u]);
#pragma unroll
        for (int tt = 0; tt < 4; ++tt) acc[u][tt] = bv; }
      acs[0][0] = acs[0][1] = bf4(bias[6]); acs[1][0] = acs[1][1] = bf4(bias[7]);
      bf16x8 wfr[2][8], xfr[2][4];
      auto load_frags = [&](auto bc, unsigned wso, unsigned xso) {
        constexpr int bs = decltype(bc)::v; constexpr unsigned kx = bs ? 64u : 0u;
        xfr[bs][0] = lds_frag((xA ^ kx) + xso); xfr[bs][1] = lds_frag((xA ^ kx) + xso + 2048);
        xfr[bs][2] = lds_frag((xB ^ kx) + xso); xfr[bs][3] = lds_frag((xB ^ kx) + xso + 2048);
#pragma unroll
        for (int u = 0; u < 8; ++u) { unsigned a = (u < 6 ? ((u & 1) ? wbO : wbE) : ((u & 1) ? wsO : wsE)) + 32 * u + wso; wfr[bs][u] = tr_frag(a, a + 1792); }
      };
      auto mma = [&](auto bc) {
        constexpr int bs = decltype(bc)::v;
#pragma unroll
        for (int u = 0; u < 6; ++u)
#pragma unroll
          for (int tt = 0; tt < 4; ++tt) acc[u][tt] = mfma(wfr[bs][u], xfr[bs][tt], acc[u][tt]);
#pragma unroll
        for (int u = 0; u < 2; ++u)
#pragma unroll
          for (int tt = 0; tt < 2; ++tt) acs[u][tt] = mfma(wfr[bs][6 + u], xfr[bs][tt], acs[u][tt]);
      };
#pragma unroll
      for (int u = 0; u < 6; ++u) asm volatile("" : "+v"(acc[u][0]), "+v"(acc[u][1]), "+v"(acc[u][2]), "+v"(acc[u][3]));
      asm volatile("" : "+v"(acs[0][0]), "+v"(acs[0][1]), "+v"(acs[1][0]), "+v"(acs[1][1]), "+v"(flagp));
      lgkm_bar();
      load_frags(IC<0>{}, WS0, XS0);
      unsigned xso = XS0;
#pragma unroll 1
      for (int j = 0; j < 7; ++j) {
        touch(j);
        lgkm_bar();
        load_frags(IC<1>{}, (j & 1) ? WS3 : WS1, xso);
        mma(IC<0>{});
        interleave<28, 20>();
        __builtin_amdgcn_sched_barrier(0);
        xso = next_x(xso);
        lgkm_bar();
        load_frags(IC<0>{}, (j & 1) ? WS0 : WS2, xso);
        mma(IC<1>{});
        interleave<28, 20>();
        __builtin_amdgcn_sched_barrier(0);
      }
      const i32x4 rst = make_rsrc(ws + ((size_t)((2 * grp + half) * KS2 + 7 * sub) * 4) * 1024, 7 * 4096);
      const unsigned sA = 2048 * wf, sB = 2048 * (1 - wf);
      auto act4 = [&](f32x4 a) { f32x4 r = {relu(a[0]), relu(a[1]), relu(a[2]), relu(a[3])}; return r * r; };
#pragma unroll
      for (int n = 0; n < 12; ++n)
        bstore16_dev(pack8(act4(acc[2 * (n >> 2)][n & 3]), act4(acc[2 * (n >> 2) + 1][n & 3])), l * 16, rst,
                     ((n & 3) < 2 ? sA : sB) + wf * 16384 + (n >> 2) * 4096 + (n & 1) * 1024);
#pragma unroll
      for (int n = 0; n < 2; ++n) bstore16_dev(pack8(act4(acs[0][n]), act4(acs[1][n])), l * 16, rst, sA + 3 * 4096 + n * 1024);
    } else {
      const int hh = w & 1;
      unsigned wb[2];
#pragma unroll
      for (int c = 0; c < 2; ++c) { int u = 7 * hh + 4 * c + p; int sw = 2 * (q >> 1) + 4 * (g & 1);
        wb[c] = lds0 + g * 3072 + q * 128 + (((u >> 1) ^ sw) << 4) + 8 * (u & 1); }
      const unsigned xr = lds0 + (64 * half + t) * 128 + ((g ^ xsw) << 4);
      const unsigned xh = half ? xr - 2048 : lds0 + 16384 + (t & 7) * 128 + ((g ^ (t & 6)) << 4);
      const unsigned xG = lds0 + (16 * sub + t) * 128 + ((g ^ xsw) << 4);
      f32x4 aq[2][4], ak[2][5], av[2][5], accg = zero4;
#pragma unroll
      for (int c = 0; c < 2; ++c) {
#pragma unroll
        for (int tt = 0; tt < 5; ++tt) { ak[c][tt] = zero4; av[c][tt] = zero4; if (tt < 4) aq[c][tt] = zero4; }
      }
      bf16x8 wfr[2][6], xfr[2][5], gfr[2], gxf[2];
      auto load_frags = [&](auto bc, unsigned wso, unsigned xso, unsigned gso) {
        constexpr int bs = decltype(bc)::v; constexpr unsigned kx = bs ? 64u : 0u;
        xfr[bs][0] = lds_frag((xh ^ kx) + xso);
#pragma unroll
        for (int tt = 0; tt < 4; ++tt) xfr[bs][tt + 1] = lds_frag((xr ^ kx) + xso + tt * 2048);
#pragma unroll
        for (int pt = 0; pt < 3; ++pt)
#pragma unroll
          for (int c = 0; c < 2; ++c) { unsigned a = wb[c] + wso + pt * 1024; wfr[bs][pt * 2 + c] = tr_frag(a, a + 512); }
        gfr[bs] = lds_frag(lds0 + LDS_G + gso + 16 * g);
        gxf[bs] = lds_frag((xG ^ kx) + xso);
      };
      auto mma = [&](auto bc) {
        constexpr int bs = decltype(bc)::v;
#pragma unroll
        for (int c = 0; c < 2; ++c) {
#pragma unroll
          for (int tt = 0; tt < 5; ++tt) {
            ak[c][tt] = mfma(wfr[bs][2 + c], xfr[bs][tt], ak[c][tt]);
            av[c][tt] = mfma(xfr[bs][tt], wfr[bs][4 + c], av[c][tt]);
            if (tt > 0) aq[c][tt - 1] = mfma(wfr[bs][c], xfr[bs][tt], aq[c][tt - 1]);
          }
        }
        accg = mfma(gfr[bs], gxf[bs], accg);
      };
#pragma unroll
      for (int c = 0; c < 2; ++c) {
        asm volatile("" : "+v"(ak[c][0]), "+v"(ak[c][1]), "+v"(ak[c][2]), "+v"(ak[c][3]), "+v"(ak[c][4]), "+v"(av[c][0]), "+v"(av[c][1]), "+v"(av[c][2]));
        asm volatile("" : "+v"(av[c][3]), "+v"(av[c][4]), "+v"(aq[c][0]), "+v"(aq[c][1]), "+v"(aq[c][2]), "+v"(aq[c][3]));
      }
      asm volatile("" : "+v"(accg), "+v"(flagp));
      lgkm_bar();
      load_frags(IC<0>{}, WS0, XS0, 0);
      unsigned xso = XS0;
#pragma unroll 1
      for (int j = 0; j < 7; ++j) {
        touch(j);
        lgkm_bar();
        load_frags(IC<1>{}, (j & 1) ? WS3 : WS1, xso, 128 * j + 64);
        mma(IC<0>{});
        interleave<29, 19>();
        __builtin_amdgcn_sched_barrier(0);
        xso = next_x(xso);
        lgkm_bar();
        load_frags(IC<0>{}, (j & 1) ? WS0 : WS2, xso, j < 6 ? 128 * j + 128 : 0);
        mma(IC<1>{});
        interleave<29, 19>();
        __builtin_amdgcn_sched_barrier(0);
      }
      if (hh == 0 && half == (sub >> 2) && g < 2)
        __hip_atomic_store((GLBP(float))(ws + WS_GATE) + (grp * 8 + sub) * 32 + l, __builtin_amdgcn_rcpf(1.f + __expf(-accg[0])),
                           __ATOMIC_RELAXED, __HIP_MEMORY_SCOPE_AGENT);
      const float sc2 = 0.18898223650461362f * 1.4426950408889634f;
      const float NEG = -1.0e30f;
      bool sel[4]; f32x4 mk2, mk0;
#pragma unroll
      for (int j = 0; j < 4; ++j) { const int d2 = t - 4 * g - j; sel[j] = d2 <= -9; mk2[j] = (unsigned)d2 <= 7u ? 0.f : NEG; mk0[j] = ((grp < 2) && (half == 0)) ? NEG : 0.f; }
      const unsigned m01 = (sel[0] ? 0xffffu : 0u) | (sel[1] ? 0xffff0000u : 0u), m23 = (sel[2] ? 0xffffu : 0u) | (sel[3] ? 0xffff0000u : 0u);
      u32x4 kf[5]; f32x4 s1[4], s2[4];
#pragma unroll
      for (int tt = 0; tt < 5; ++tt) kf[tt] = pack8(ak[0][tt], ak[1][tt]);
#pragma unroll
      for (int tt = 0; tt < 4; ++tt) {
        u32x4 qu = pack8(aq[0][tt], aq[1][tt]);
        qu[2] = g == 3 ? 0u : qu[2]; qu[3] = g == 3 ? 0u : qu[3];
        s1[tt] = mfma(asb(kf[tt]), asb(qu), tt == 0 ? mk0 : zero4); s2[tt] = mfma(asb(kf[tt + 1]), asb(qu), mk2);
      }
      u32x2 vp[2][5];
#pragma unroll
      for (int c = 0; c < 2; ++c)
#pragma unroll
        for (int tt = 0; tt < 5; ++tt) vp[c][tt] = u32x2{pk2(av[c][tt][0], av[c][tt][1]), pk2(av[c][tt][2], av[c][tt][3])};
      const u32x4 ones = {0x3f803f80u, 0x3f803f80u, 0x3f803f80u, 0x3f803f80u};
      f32x4 o0[4], o1[4], sm[4];
#pragma unroll
      for (int tt = 0; tt < 4; ++tt) {
        f32x4 sc;
#pragma unroll
        for (int j = 0; j < 4; ++j) sc[j] = sel[j] ? s1[tt][j] : s2[tt][j];
        const float m = xmax32(xmax16(max3(max3(sc[0], sc[1], sc[2]), sc[3], -3.0e38f)));
        const float ms = -m * sc2;
#pragma unroll
        for (int j = 0; j < 4; ++j) sc[j] = __builtin_amdgcn_exp2f(__builtin_fmaf(sc[j], sc2, ms));
        const unsigned e01 = pk2(sc[0], sc[1]), e23 = pk2(sc[2], sc[3]);
        const bf16x8 pf = asb(u32x4{e01 & m01, e23 & m23, e01 & ~m01, e23 & ~m23});
        const u32x4 v0 = {vp[0][tt].x, vp[0][tt].y, vp[0][tt + 1].x, vp[0][tt + 1].y}, v1 = {vp[1][tt].x, vp[1][tt].y, vp[1][tt + 1].x, vp[1][tt + 1].y};
        sm[tt] = mfma(asb(ones), pf, zero4);
        o0[tt] = mfma(asb(v0), pf, zero4); o1[tt] = mfma(asb(v1), pf, zero4);
      }
      const i32x4 rst = make_rsrc(ws + ((size_t)((2 * grp + half) * KS2 + 56 + 2 * sub + hh) * 4) * 1024, 4096);
#pragma unroll
      for (int tt = 0; tt < 4; ++tt) {
        const float inv = __builtin_amdgcn_rcpf(sm[tt][0]);
        u32x4 ou = pack8(o0[tt] * inv, o1[tt] * inv);
        ou[2] = g == 3 ? 0u : ou[2]; ou[3] = g == 3 ? 0u : ou[3];
        bstore16_dev(ou, l * 16, rst, tt * 1024);
      }
    }
    asm volatile("" : "+v"(tagv)); tagv += 1;
    if (!has2) { wait_vm<0>(); if (l < 7) st_dev(flagp, tagv); return; }
  }

  const int kq = (wv & 1) + 2 * (wv >> 2), th2 = (wv >> 1) & 1;
  unsigned bb[4];
  {
    const int rr = 4 * g + q;
#pragma unroll
    for (int nt = 0; nt < 4; ++nt) bb[nt] = lds0 + kq * 4096 + rr * 128 + (((2 * nt + (p >> 1)) ^ (((rr >> 1) & 3) << 1)) << 4) + 8 * (p & 1);
  }
  auto load_f = [&](bf16x8 (&bf)[4], unsigned off) {
#pragma unroll
    for (int nt = 0; nt < 4; ++nt) bf[nt] = tr_frag(bb[nt] + off, bb[nt] + off + 2048);
  };
  bf16x8 br[8][4], be[4][4], bl[8][4];
  bar();
#pragma unroll
  for (int i = 0; i < 8; ++i) load_f(br[i], i * 16384);
  lgkm_bar();
  u32x2 b2v[4] = {u32x2{0u, 0u}, u32x2{0u, 0u}, u32x2{0u, 0u}, u32x2{0u, 0u}};
  if (older) {
    wait_vm<0>();
    if (l < 7) st_dev(flagp, tagv);
    if (kq == 1) {
      const bf16_t* bp = b2 + ctile * 64 + 4 * g;
#pragma unroll
      for (int nt = 0; nt < 4; ++nt) b2v[nt] = *(const u32x2*)(bp + 16 * nt);
      asm volatile("" : "+v"(b2v[0]), "+v"(b2v[1]), "+v"(b2v[2]), "+v"(b2v[3]));
    }
  } else {
    issue_b(40, 32);
  }
  f32x4 acc[4][2], acch[4][2];
#pragma unroll
  for (int nt = 0; nt < 4; ++nt) acc[nt][0] = acc[nt][1] = bf4(b2v[nt]);
  const i32x4 rsa = make_rsrc(ws + (size_t)rbk * KS2 * 4096, KS2 * 4096);
  const unsigned ao = (unsigned)(th2 * 2048 + l * 16);
  const unsigned aR = (unsigned)kq * 4096u;
  const unsigned aS = (unsigned)(older ? 40 + kq : 54 + kq) * 4096u;
  const unsigned aN = older ? (unsigned)(32 + kq) * 4096u : aS;
  unsigned wad = lds0 + (older ? 131072 : 0) + (kq & 1) * 16384 + th2 * 8192 + ((l ^ ((g >> 1) << 2)) * 16);
  unsigned wad2 = wad ^ 128, rad; bf16_t* op;
  const float* gp = (const float*)(ws + WS_GATE) + (rbk * 4 + th2 * 2) * 32 + t;
  {
    const int r = threadIdx.x >> 3, c8 = threadIdx.x & 7;
    rad = lds0 + ((r >> 4) * 4 + (c8 >> 1)) * 1024 + ((c8 & 1) * 32 + ((r & 15) ^ ((c8 & 3) << 2))) * 16;
    const int g2 = rbk >> 1, i2 = (rbk & 1) * 64 + r;
    op = out + (size_t)((g2 >> 1) * 256 + (g2 & 1) + 2 * i2) * D + ctile * 64 + c8 * 8;
    asm volatile("" : "+v"(wad), "+v"(rad), "+v"(wad2), "+v"(op), "+v"(gp));
  }
  const u64 mytag = __shfl(tagv, 63);
  if (wv == 0) wait_flags((const u64*)(ws + WS_MAIL) + cons * 32 + (l & 31), mytag);
  __builtin_amdgcn_s_setprio(0);
  bar();
  float gt0, gt1;
  asm volatile("global_load_dword %0, %2, off sc1\n\tglobal_load_dword %1, %2, off offset:128 sc1" : "=&v"(gt0), "=&v"(gt1) : "v"(gp) : "memory");
  u32x4 af[4][2];
#pragma unroll
  for (int i = 0; i < 4; ++i) aload2(af[i][0], af[i][1], ao, rsa, aR + i * 16384);
  auto step = [&](auto rc, auto wc, bf16x8 (&bf)[4]) {
    constexpr int r = decltype(rc)::v;
    wait_a2<decltype(wc)::v>(af[r][0], af[r][1]);
#pragma unroll
    for (int nt = 0; nt < 4; ++nt)
#pragma unroll
      for (int tt = 0; tt < 2; ++tt) acc[nt][tt] = mfma(bf[nt], asb(af[r][tt]), acc[nt][tt]);
    __builtin_amdgcn_sched_barrier(0);
  };
  static_for<0, 8>([&](auto ic) {
    constexpr int i = decltype(ic)::v;
    if constexpr (i == 7) { if (!older) bar(); }
    if constexpr (i >= 4) { if (older) load_f(be[i - 4], 131072 + (i - 4) * 8192); }
    step(IC<i & 3>{}, IC<6>{}, br[i]);
    aload2(af[i & 3][0], af[i & 3][1], ao, rsa, i < 4 ? aR + (i + 4) * 16384 : aN + (i - 4) * 8192);
  });
  if (older) {
    static_for<0, 4>([&](auto ic) {
      constexpr int i = decltype(ic)::v;
      if constexpr (i == 3) bar();
      step(ic, IC<6>{}, be[i]);
      aload2(af[i][0], af[i][1], ao, rsa, aS + i * 8192);
    });
  }
  load_f(bl[0], 0); load_f(bl[1], 16384);
  asm volatile("" : "+v"(gt0), "+v"(gt1));
#pragma unroll
  for (int nt = 0; nt < 4; ++nt) { acch[nt][0] = acc[nt][0] * (1.f - gt0); acch[nt][1] = acc[nt][1] * (1.f - gt1); acc[nt][0] = zero4; acc[nt][1] = zero4; }
  const float gt[2] = {older ? 1.f - gt0 : gt0, older ? 1.f - gt1 : gt1};
  static_for<0, 8>([&](auto ic) {
    constexpr int i = decltype(ic)::v;
    if constexpr (i < 3) { load_f(bl[2 * i + 2], (2 * i + 2) * 16384); load_f(bl[2 * i + 3], (2 * i + 3) * 16384); }
    if constexpr (i == 5) lgkm_bar();
    step(IC<i & 3>{}, IC<(i < 4 ? 6 : 2 * (7 - i))>{}, bl[i]);
    if constexpr (i < 4) aload2(af[i][0], af[i][1], ao, rsa, aS + (i + 4) * 8192);
  });
#pragma unroll
  for (int nt = 0; nt < 4; ++nt)
#pragma unroll
    for (int tt = 0; tt < 2; ++tt) *(LDSP(f32x4))(uintptr_t)(((nt & 1) ? wad2 : wad) + (tt * 4 + nt) * 1024) = acch[nt][tt] + acc[nt][tt] * gt[tt];
  lgkm_bar();
  {
    const f32x4 v0 = (lds_f4(rad) + lds_f4(rad + 16384)) + (lds_f4(rad + 131072) + lds_f4(rad + 147456));
    const f32x4 v1 = (lds_f4(rad + 256) + lds_f4(rad + 16640)) + (lds_f4(rad + 131328) + lds_f4(rad + 147712));
    const u32x4 ov = pack8(v0, v1);
    asm volatile("global_store_dwordx4 %0, %1, off sc1\n\ts_nop 1" :: "v"(op), "v"(ov) : "memory");
  }
  if (threadIdx.x == 0) st_dev((u64*)(ws + WS_EPOCH) + cons * 16, mytag);
}

extern "C" void kernel_launch(void* const* d_in, const int* in_sizes, int n_in, void* d_out, int out_size, void* d_ws, size_t ws_size, hipStream_t stream) {
  const bf16_t* x = (const bf16_t*)d_in[0]; const bf16_t* wqkv = (const bf16_t*)d_in[1]; const bf16_t* wo = (const bf16_t*)d_in[2];
  const bf16_t* w1 = (const bf16_t*)d_in[4]; const bf16_t* b1 = (const bf16_t*)d_in[5]; const bf16_t* w2 = (const bf16_t*)d_in[6];
  const bf16_t* b2 = (const bf16_t*)d_in[7]; const bf16_t* gw = (const bf16_t*)d_in[8];
  fused_kernel<<<256, 512, 0, stream>>>(x, wqkv, wo, w1, b1, w2, b2, gw, (char*)d_ws, (bf16_t*)d_out);
}
```
